```python
import torch

def _softmax(x, axis=-1):
    return torch.softmax(x, dim=axis)
def _gelu(x):
    return torch.nn.functional.gelu(x, approximate="tanh")
def _silu(x):
    return torch.nn.functional.silu(x)
def _sigmoid(x):
    return torch.sigmoid(x)
def _layernorm(x, axis=-1, eps=1e-5):
    m = x.mean(dim=axis, keepdim=True)
    v = x.var(dim=axis, keepdim=True, unbiased=False)
    return (x - m) / torch.sqrt(v + eps)
def _scatter_add(tgt, idx, src, axis=0):
    return tgt.clone().index_add_(axis, idx, src)
def _segment_sum(src, seg, n):
    out = torch.zeros((n,) + src.shape[1:], dtype=src.dtype)
    return out.index_add_(0, seg, src)
def _pad(x, pw):
    flat = tuple(v for pair in reversed(pw) for v in pair)
    return torch.nn.functional.pad(x, flat)

kernel_name = 'sequential_preLN__compressed_kv_cla__dense_activation_relu__decode'

def setup_inputs():

    _w = (lambda a: (lambda t: t.to(torch.bfloat16) if t.is_floating_point() else t)(torch.as_tensor(a)))
    def _A_setup():
        B, H, Sq, Sk, D, M = 1, 32, 1, 8192, 128, 640
        Dc = 4 * D
        return {
            "x": _w((lambda *s: torch.randn(*s, dtype=torch.bfloat16) * 0.05)(B, Sq, M)),
            "c_kv": _w((lambda *s: torch.randn(*s, dtype=torch.bfloat16) * 0.4)(B, Sk, Dc)),
            "w_q": _w((lambda *s: torch.randn(*s, dtype=torch.bfloat16) * 3.2)(M, H, D) * (M ** -0.5)),
            "w_uk": _w((lambda *s: torch.randn(*s, dtype=torch.bfloat16) * 3.2)(Dc, H, D) * (Dc ** -0.5)),
            "w_uv": _w((lambda *s: torch.randn(*s, dtype=torch.bfloat16) * 0.4)(Dc, H, D) * (Dc ** -0.5)),
            "w_o": _w((lambda *s: torch.randn(*s, dtype=torch.bfloat16) * 0.4)(H, D, M) * ((H * D) ** -0.5)),
        }
    _A_in = _A_setup()

    def _B_setup():
        B, S, D, Dff = 1, 1, 640, 1728
        return {
            "x": _w((lambda *s: torch.randn(*s, dtype=torch.bfloat16) * 0.05)(B, S, D)),
            "w1": _w((lambda *s: torch.randn(*s, dtype=torch.bfloat16) * 0.05)(D, Dff)),
            "w2": _w((lambda *s: torch.randn(*s, dtype=torch.bfloat16) * 0.05)(Dff, D)),
        }
    _B_in = _B_setup()

    return {
        "A_x": _A_in["x"],
        "A_c_kv": _A_in["c_kv"],
        "A_w_q": _A_in["w_q"],
        "A_w_uk": _A_in["w_uk"],
        "A_w_uv": _A_in["w_uv"],
        "A_w_o": _A_in["w_o"],
        "B_x": _B_in["x"],
        "B_w1": _B_in["w1"],
        "B_w2": _B_in["w2"],
    }

def reference(A_x, A_c_kv, A_w_q, A_w_uk, A_w_uv, A_w_o, B_x, B_w1, B_w2):
    def _A_ref(x, c_kv, w_q, w_uk, w_uv, w_o):
        B, Sq, M = x.shape[0], x.shape[1], x.shape[2]
        Sk = c_kv.shape[1]
        H, D = w_q.shape[1], w_q.shape[2]
        q = torch.einsum("bqm,mhd->bhqd", x, w_q)
        k = torch.einsum("bkc,chd->bhkd", c_kv, w_uk)
        v = torch.einsum("bkc,chd->bhkd", c_kv, w_uv)
        scores = torch.einsum("bhqd,bhkd->bhqk", q, k) * (D ** -0.5)
        off = Sk - Sq
        qi = torch.arange(Sq)[:, None]
        kj = torch.arange(Sk)[None, :]
        cmask = kj <= (qi + off)
        scores = torch.where(cmask[None, None], scores, -1e9)
        p = _softmax(scores, axis=-1)
        ctx = torch.einsum("bhqk,bhkd->bhqd", p, v)
        return torch.einsum("bhqd,hdm->bqm", ctx, w_o)

    def _B_ref(x, w1, w2):
        h = torch.einsum("bsd,df->bsf", x, w1)
        zero = torch.zeros_like(h)
        h = torch.maximum(h, zero)
        return torch.einsum("bsf,fd->bsd", h, w2)

    x = A_x
    h = x + _A_ref(_layernorm(x, axis=-1), A_c_kv, A_w_q, A_w_uk, A_w_uv, A_w_o)
    h2 = h + _B_ref(_layernorm(h, axis=-1), B_w1, B_w2)
    return h2
```

```cpp
#include <hip/hip_runtime.h>
#include <stdint.h>

namespace {

typedef unsigned v4u __attribute__((ext_vector_type(4)));
typedef unsigned v2u __attribute__((ext_vector_type(2)));
typedef float f4 __attribute__((ext_vector_type(4)));
typedef float f2 __attribute__((ext_vector_type(2)));
typedef short s8v __attribute__((ext_vector_type(8)));
typedef short s4v __attribute__((ext_vector_type(4)));
typedef __bf16 bf2 __attribute__((ext_vector_type(2)));
typedef unsigned short u16;
#define GAS __attribute__((address_space(1)))
#define LAS __attribute__((address_space(3)))
#define DI __device__ __forceinline__

constexpr int H1_DELAY = 8;
constexpr unsigned F2_T = 140;
constexpr int ML_DELAY = 13;
constexpr int S4_DELAY = 10;
constexpr int FIN_DELAY = 30;
constexpr int CODE_KB = 21;

constexpr unsigned CF_OFF = 1024;
constexpr unsigned H1_OFF = 4096;
constexpr unsigned H2_OFF = H1_OFF + 32 * 4 * 128 * 8;
constexpr unsigned ML_OFF = H2_OFF + 8 * 32 * 1024;
constexpr unsigned LG_OFF = ML_OFF + 64 * 4 * 32 * 8;
constexpr unsigned H4_OFF = ML_OFF + 64 * 4 * 32 * 16;
constexpr unsigned H5_OFF = H4_OFF + 32 * 4 * 256;
constexpr unsigned H6_OFF = H5_OFF + 8 * 8 * 1280;
constexpr unsigned H6_STRIDE = 7168;
constexpr unsigned F2_OFF = H6_OFF + 20 * H6_STRIDE;
constexpr unsigned CA_OFF = F2_OFF + 128 * 128;
constexpr unsigned H3_OFF = CA_OFF + 128 * 128;
constexpr unsigned WS_USED = H3_OFF + 64 * 32 * 1024;
static_assert(H3_OFF % 1024 == 0 && H6_OFF % 256 == 0, "alignment");

constexpr unsigned SENT = 0xFFFFFFFFu;
constexpr unsigned SPIN_MAX = 1u << 17;
constexpr int AUX_SC1 = 16;

constexpr int RS = 1056;
constexpr int L_CKV = 0;
constexpr int L_Q = 128 * RS;
constexpr int L_P = L_Q + 16 * RS;
constexpr int L_M = L_P + 4096;
constexpr int L_X = L_M + 256;
constexpr int L_LS = L_X + 256;
constexpr int L_JUNK = L_X + 1536;
constexpr int LDS_TOTAL = L_JUNK + 1024;
constexpr int L_R1 = L_P;
constexpr int L_WUV = 0;
constexpr int L_O = 32768;
constexpr int L_LQ = 40960;
constexpr int L_ML = L_O + 16 * 288;
constexpr int L_WO = 0;
constexpr int L_W2 = 0;
constexpr int L_W1 = 81920;
constexpr int L_QS = L_M;
constexpr int L_R5 = 147456;
constexpr int L_S = 147456;
constexpr int L_C = L_S + 2048;
constexpr int L_H = 147456;
static_assert(L_C + 2048 <= L_P, "lds");

DI float bf_lo(unsigned u) { return __uint_as_float(u << 16); }
DI float bf_hi(unsigned u) { return __uint_as_float(u & 0xffff0000u); }
DI unsigned pack_bf(float a, float b) { f2 v = {a, b}; return __builtin_bit_cast(unsigned, __builtin_convertvector(v, bf2)); }
template <int CTRL> DI float dpp(float v) { return __int_as_float(__builtin_amdgcn_update_dpp(0, __float_as_int(v), CTRL, 0xf, 0xf, true)); }
constexpr int QP_X1 = 0xB1, QP_X2 = 0x4E, ROR4 = 0x124, ROR8 = 0x128, HMIR = 0x141, MIR = 0x140;
DI float tsum32(float a, float b) { auto r = __builtin_amdgcn_permlane32_swap(__float_as_uint(a), __float_as_uint(b), false, false); return __uint_as_float(r[0]) + __uint_as_float(r[1]); }
DI float tsum16(float a, float b) { auto r = __builtin_amdgcn_permlane16_swap(__float_as_uint(a), __float_as_uint(b), false, false); return __uint_as_float(r[0]) + __uint_as_float(r[1]); }
DI float fmx(float a, float b) { return __builtin_elementwise_maximum(a, b); }
DI float xmax32(float a) { auto r = __builtin_amdgcn_permlane32_swap(__float_as_uint(a), __float_as_uint(a), false, false); return fmx(__uint_as_float(r[0]), __uint_as_float(r[1])); }
DI float xmax16(float a) { auto r = __builtin_amdgcn_permlane16_swap(__float_as_uint(a), __float_as_uint(a), false, false); return fmx(__uint_as_float(r[0]), __uint_as_float(r[1])); }
DI float row_sum(float v) { v += dpp<QP_X1>(v); v += dpp<QP_X2>(v); v += dpp<HMIR>(v); v += dpp<MIR>(v); return v; }
DI float wave_sum(float v) { v = row_sum(v); v = tsum16(v, v); return tsum32(v, v); }
DI float wave_max(float v) {
  v = fmx(v, dpp<QP_X1>(v)); v = fmx(v, dpp<QP_X2>(v)); v = fmx(v, dpp<HMIR>(v)); v = fmx(v, dpp<MIR>(v));
  return xmax32(xmax16(v));
}
DI float dot2(unsigned a, unsigned b, float c) { return __builtin_amdgcn_fdot2_f32_bf16(__builtin_bit_cast(bf2, a), __builtin_bit_cast(bf2, b), c, false); }
DI f4 mfma(v4u a, v4u b, f4 c) { return __builtin_amdgcn_mfma_f32_16x16x32_bf16(__builtin_bit_cast(s8v, a), __builtin_bit_cast(s8v, b), c, 0, 0, 0); }
DI void dma16(const void* sbase, unsigned voff, unsigned lds_addr) {
  unsigned keep;
  asm volatile("s_mov_b32 %0, m0\n\ts_mov_b32 m0, %3\n\ts_nop 0\n\tglobal_load_lds_dwordx4 %1, %2\n\ts_mov_b32 m0, %0"
               : "=&s"(keep) : "v"(voff), "s"(sbase), "s"(lds_addr) : "memory");
}
DI void dma16k(const void* sbase, unsigned voff, unsigned lds_addr) {
  unsigned keep;
  asm volatile("s_mov_b32 %0, m0\n\ts_mov_b32 m0, %3\n\ts_nop 0\n\tglobal_load_lds_dwordx4 %1, %2 nt\n\ts_mov_b32 m0, %0"
               : "=&s"(keep) : "v"(voff), "s"(sbase), "s"(lds_addr) : "memory");
}
DI void touch16(const void* sbase, unsigned voff, unsigned lds_addr) {
  unsigned keep;
  asm volatile("s_mov_b32 %0, m0\n\ts_mov_b32 m0, %3\n\ts_nop 0\n\tglobal_load_lds_dwordx4 %1, %2 sc1\n\ts_mov_b32 m0, %0"
               : "=&s"(keep) : "v"(voff), "s"(sbase), "s"(lds_addr) : "memory");
}
DI v2u tr_read(LAS char* p) { return __builtin_bit_cast(v2u, __builtin_amdgcn_ds_read_tr16_b64_v4i16((LAS s4v*)p)); }
#define BARRIER() asm volatile("s_waitcnt lgkmcnt(0)\n\ts_barrier" ::: "memory")
#define VMWAIT(n) asm volatile("s_waitcnt vmcnt(" #n ")" ::: "memory")
#define PIN() asm volatile("" ::: "memory")
#define SCHED() __builtin_amdgcn_sched_barrier(0)
#define POLL1(T, res, LOAD, BAD, SLP) T res; for (unsigned sp_i = 0;; ++sp_i) { T& x = res; PIN(); LOAD; \
    if (__builtin_expect(__builtin_amdgcn_ballot_w64((BAD) != 0u) == 0ull, 1)) break; \
    PIN(); if (sp_i > SPIN_MAX) break; if (SLP) __builtin_amdgcn_s_sleep(SLP); }
#define KEEP(x) asm volatile("" : "+v"(x))
#define NOPK(x) asm("" : "+v"(x))
struct v4x2 { v4u a, b; };
struct v4x4 { v4u a, b, c, d; };

struct Params {
  const u16 *x, *ckv, *wq, *wuk, *wuv, *wo, *w1, *w2;
  u16* out;
  char* ws;
};
#define LD32(off) __builtin_amdgcn_raw_buffer_load_b32(ws, (off), 0, AUX_SC1)
#define LD64(off) __builtin_amdgcn_raw_buffer_load_b64(ws, (off), 0, AUX_SC1)
#define LD128(off) __builtin_amdgcn_raw_buffer_load_b128(ws, (off), 0, AUX_SC1)
#define ST32(v, off) __builtin_amdgcn_raw_buffer_store_b32((v), ws, (off), 0, AUX_SC1)
#define ST64(v, off) __builtin_amdgcn_raw_buffer_store_b64((v), ws, (off), 0, AUX_SC1)
#define ST128(v, off) __builtin_amdgcn_raw_buffer_store_b128((v), ws, (off), 0, AUX_SC1)
#define ALD128(dst, base, off) asm volatile("global_load_dwordx4 %0, %1, %2" : "=v"(dst) : "v"(off), "s"(base) : "memory")
#define WAIT1(n, a) asm volatile("s_waitcnt vmcnt(" #n ")" : "+v"(a) :: "memory")
#define WAIT2(n, a, b) asm volatile("s_waitcnt vmcnt(" #n ")" : "+v"(a), "+v"(b) :: "memory")
#define WAIT4(n, a) asm volatile("s_waitcnt vmcnt(" #n ")" : "+v"(a[0]), "+v"(a[1]), "+v"(a[2]), "+v"(a[3]) :: "memory")
DI unsigned make_tag(unsigned epoch) { return (((epoch + 1u) * 0x9E3779B1u) ^ 0x5BD1E996u) & ~1u; }
DI unsigned umax(unsigned a, unsigned b) { return a > b ? a : b; }
DI unsigned max4(v4u a) { return umax(umax(a.x, a.y), umax(a.z, a.w)); }
DI unsigned sent4(v4u a) { return max4(a) == SENT; }
DI unsigned low_from_high(unsigned old, unsigned src) { return __builtin_amdgcn_update_dpp(old, src, ROR8, 0xf, 0x3, false); }
DI float sum4(f4 d) { const f2 s = (f2){d.x, d.y} + (f2){d.z, d.w}; return s.x + s.y; }
DI v4u frag(LAS char* a, int hi_off) { const v2u lo = tr_read(a), hi = tr_read(a + hi_off); return (v4u){lo.x, lo.y, hi.x, hi.y}; }
constexpr unsigned OOB = 0x7ffffff0u;

__global__ void __launch_bounds__(512, 2) mla_mlp_decode(Params p) {
  __shared__ __attribute__((aligned(16))) char smem[LDS_TOTAL];
  LAS char* L = (LAS char*)smem;
  const unsigned lds0 = (unsigned)(uintptr_t)L;
  const int tid = threadIdx.x, lane = tid & 63, wave = __builtin_amdgcn_readfirstlane(tid >> 6);
  const int b = blockIdx.x, xc = b & 7, r = b >> 3;
  const int g = lane >> 4, i16 = lane & 15;
  unsigned long long pc0;
  asm volatile("s_getpc_b64 %0" : "=s"(pc0));
  pc0 &= ~255ull;
  if (r >= 16 && wave >= 6 && (r - 16) * 2 + wave - 6 < CODE_KB) touch16((const void*)pc0, ((r - 16) * 2 + wave - 6) * 1024 + lane * 16, lds0 + L_JUNK);
  const bool isq = r < 16;
  const int r16 = r & 15;
  const int h1 = (xc & 1) * 16 + r16, j4 = xc >> 1;
  const int sp_ = 8 * xc + (r16 >> 1), hg = r16 & 1;
  unsigned epv;
  const __amdgpu_buffer_rsrc_t ws = __builtin_amdgcn_make_buffer_rsrc(p.ws, 0, WS_USED, 0x00020000);
  const f4 z4 = {0.f, 0.f, 0.f, 0.f};
  constexpr unsigned ONE2 = 0x3f803f80u;

  if (!isq && wave < 4) {
    unsigned go = (unsigned)(128 * sp_ + 32 * wave) * 1024u + lane * 16, lo = lds0 + L_CKV + 32 * wave * RS;
    unsigned so = H3_OFF + ((sp_ * 4 + wave) * 32 + hg * 16 + 4 * g) * 256 + i16 * 16;
    const v4u sent = {SENT, SENT, SENT, SENT};
#pragma unroll 1
    for (int i = 0; i < 16; ++i) {
      dma16k(p.ckv, go, lo); dma16k(p.ckv, go + 1024, lo + RS);
      go += 2048; lo += 2 * RS;
      if (i >= 3 && i < 7) { ST128(sent, so); so += 256; }
      if (i == 7) {
        ST32(SENT, lane < 16 ? H4_OFF + (h1 * 4 + j4) * 256 + wave * 64 + lane * 4 : OOB);
        ST64(((v2u){SENT, SENT}), wave < 2 ? (wave ? LG_OFF : ML_OFF) + ((sp_ * 4 + g) * 32 + hg * 16 + i16) * 8 : OOB);
      }
    }
  }

  if (__builtin_expect(isq, 1)) {
    const int row0 = 160 * j4 + 20 * wave + g;
    v4u xa, wq[5], wuk[4];
    unsigned xb;
    asm volatile("global_load_dwordx4 %0, %1, %2" : "=v"(xa) : "v"(lane * 16), "s"(p.x) : "memory");
    asm volatile("global_load_dword %0, %1, %2 offset:1024" : "=v"(xb) : "v"(lane * 4), "s"(p.x) : "memory");
#pragma unroll
    for (int i = 0; i < 5; ++i) ALD128(wq[i], p.wq, (unsigned)(row0 + 4 * i) * 8192u + h1 * 256 + i16 * 16);
    asm volatile("global_load_dword %0, %1, %2 sc1" : "=v"(epv) : "v"(0), "s"(p.ws) : "memory");
    unsigned h2off = i16 < 8 ? H2_OFF + i16 * 32768 + h1 * 1024 + j4 * 256 + wave * 32 + g * 8 : OOB;
    unsigned h1off = !(lane & 24) ? H1_OFF + (h1 * 4 + j4) * 1024 + wave * 128 + (lane & 7) * 16 + (lane >> 5) * 8 : OOB;
    unsigned r1w = lds0 + L_R1 + wave * 512 + (8 * i16 + 2 * g) * 4, r1r = lds0 + L_R1 + (lane >> 3) * 512 + wave * 64 + (lane & 7) * 8;
    unsigned wukoff = (unsigned)(128 * j4 + 16 * wave + 4 * g) * 8192u + h1 * 256 + i16 * 16;
    KEEP(h2off); KEEP(h1off); KEEP(r1w); KEEP(r1r); KEEP(wukoff);
    WAIT2(6, xa, xb);
    ST64(((v2u){SENT, SENT}), (wave == 0 && lane == 8) ? F2_OFF + b * 128 : h1off);
    *(LAS v4u*)(L + L_X + lane * 16) = xa;
    *(LAS unsigned*)(L + L_X + 1024 + lane * 4) = xb;
    PIN();
    float xr[5];
#pragma unroll
    for (int i = 0; i < 5; ++i) xr[i] = bf_lo(*(LAS u16*)(L + L_X + row0 * 2 + 8 * i));
    float acc[8];
    {
      float s1 = dot2(xa.x, ONE2, 0.f), s2 = dot2(xa.x, xa.x, 0.f);
      s1 = dot2(xa.y, ONE2, s1); s2 = dot2(xa.y, xa.y, s2); s1 = dot2(xa.z, ONE2, s1); s2 = dot2(xa.z, xa.z, s2);
      s1 = dot2(xa.w, ONE2, s1); s2 = dot2(xa.w, xa.w, s2); s1 = dot2(xb, ONE2, s1); s2 = dot2(xb, xb, s2);
      const float mean = wave_sum(s1) * (1.f / 640.f);
      const float rstd = __builtin_amdgcn_rsqf(fmx(wave_sum(s2) * (1.f / 640.f) - mean * mean, 0.f) + 1e-5f);
      unsigned n[5];
#pragma unroll
      for (int i = 0; i < 5; ++i) n[i] = pack_bf((xr[i] - mean) * rstd, 0.f);
      WAIT1(6, wq[0]);
#pragma unroll
      for (int e = 0; e < 4; ++e) { acc[2 * e] = dot2(wq[0][e], n[0], 0.f); acc[2 * e + 1] = dot2(wq[0][e], n[0] << 16, 0.f); }
#define QSTEP(i, cnt) WAIT1(cnt, wq[i]); _Pragma("unroll") for (int e = 0; e < 4; ++e) { acc[2 * e] = dot2(wq[i][e], n[i], acc[2 * e]); acc[2 * e + 1] = dot2(wq[i][e], n[i] << 16, acc[2 * e + 1]); }
      QSTEP(1, 5) QSTEP(2, 4) QSTEP(3, 3) QSTEP(4, 2)
    }
    {
      const float a0 = tsum32(acc[0], acc[4]), a1 = tsum32(acc[1], acc[5]), a2 = tsum32(acc[2], acc[6]), a3 = tsum32(acc[3], acc[7]);
      const f2 v = {tsum16(a0, a2), tsum16(a1, a3)};
      *(LAS f2*)(uintptr_t)r1w = v;
    }
    WAIT1(1, epv);
    if (wave == 0) {
      const v4u sent = {SENT, SENT, SENT, SENT};
      const unsigned o = H2_OFF + g * 32768 + h1 * 1024 + j4 * 256 + i16 * 16;
      ST128(sent, o); ST128(sent, o + 4 * 32768);
    }
#pragma unroll
    for (int i = 0; i < 4; ++i) ALD128(wuk[i], p.wuk, wukoff + i * 8192);
    const unsigned epoch = __builtin_amdgcn_readfirstlane(epv);
    const unsigned tag = make_tag(epoch);
    BARRIER();
    {
      f2 s = *(LAS f2*)(uintptr_t)r1r;
      s.x += dpp<ROR8>(s.x); NOPK(s.x); s.y += dpp<ROR8>(s.y);
      float t = tsum32(s.x, s.y);
      t = tsum16(t, t);
      const v2u v = {__float_as_uint(t), tag};
      ST64(v, h1off);
    }
    if (wave == 0) {
      const v2u fv = {(unsigned)__builtin_amdgcn_s_memrealtime(), tag};
      VMWAIT(5);
      if (lane == 0) ST64(fv, F2_OFF + b * 128);
    }
    const int li = (r - 10) * 8 + xc;
    if (li >= 27 && li < 47 && wave * 64 + lane < 440) {
      const v4u sent = {SENT, SENT, SENT, SENT};
      ST128(sent, H6_OFF + (li - 27) * H6_STRIDE + (wave * 64 + lane) * 16);
    }
    if (r < 10 && lane < 32) ST32(SENT, H5_OFF + wave * 10240 + xc * 1280 + r * 128 + lane * 4);
    {
      const unsigned off = H1_OFF + (h1 * 4 + g) * 1024 + wave * 128 + i16 * 8;
      unsigned qsw = lds0 + L_QS + (16 * wave + i16) * 2, qsr = lds0 + L_QS + i16 * 16;
      KEEP(qsw); KEEP(qsr);
      if (H1_DELAY) __builtin_amdgcn_s_sleep(H1_DELAY);
      POLL1(v2u, v, x = LD64(off), x.y ^ tag, 1);
      WAIT4(0, wuk);
      float tq = __uint_as_float(v.x);
      tq = tsum32(tq, tq);
      tq = tsum16(tq, tq) * (0.08838834764831845f * 1.4426950408889634f);
      *(LAS u16*)(uintptr_t)qsw = (u16)pack_bf(tq, tq);
      BARRIER();
      const v4u q8 = *(LAS v4u*)(uintptr_t)qsr;
      float t[4];
#pragma unroll
      for (int i = 0; i < 4; ++i) {
        float a = dot2(wuk[i].x, q8.x, 0.f); a = dot2(wuk[i].y, q8.y, a); a = dot2(wuk[i].z, q8.z, a); a = dot2(wuk[i].w, q8.w, a);
        t[i] = row_sum(a);
        NOPK(t[i]);
      }
      const v2u qv = {pack_bf(t[0], t[1]), pack_bf(t[2], t[3])};
      ST64(qv, h2off);
      if (r < 10 && wave == 0 && lane == 0) ST32(tag, CF_OFF + b * 4);
    }
    asm volatile("s_barrier" ::: "memory");
    if (!(r >= 10 && li < 27)) {
#pragma unroll
      for (int i = 0; i < 4; ++i) dma16(p.wuv, wukoff + i * 8192, lds0 + L_JUNK);
      if (r < 10) {
        const int nf = xc < 3 ? 4 : 3;
        for (int n = r * 8 + wave; n < nf * 32; n += 80)
          dma16(p.wuv, (unsigned)(128 * j4 + 4 * (n & 31) + g) * 8192u + ((xc & 1) * 16 + 10 + (n >> 5)) * 256 + i16 * 16, lds0 + L_JUNK);
      }
      asm volatile("s_barrier" ::: "memory");
    }
    const int f128 = ((i16 >> 3) & 1) | ((g & 1) << 1);
    if (r < 10) {
#pragma unroll
      for (int i = 0; i < 8; ++i)
        dma16(p.wo, (unsigned)(512 * xc + 64 * wave + 8 * i + (lane >> 3)) * 1280u + r * 128 + (((lane & 7) ^ (((g & 1) | ((i & 1) << 1)) << 1)) * 16),
              lds0 + L_WO + wave * 8192 + i * 1024);
      {
        const int hh = 4 * xc + (wave >> 1);
        POLL1(unsigned, cf, x = LD32(CA_OFF + ((hh & 1) * 64 + 8 * (2 * (lane & 3) + (hh >> 4)) + ((hh & 15) >> 1)) * 128), x ^ tag, 8);
      }
      VMWAIT(0);
      v4u wo[2][4];
#pragma unroll
      for (int s = 0; s < 2; ++s)
#pragma unroll
        for (int t = 0; t < 4; ++t) wo[s][t] = frag(L + L_WO + wave * 8192 + (32 * s + 8 * g + (i16 >> 2)) * 128 + (t ^ f128) * 32 + (i16 & 3) * 8, 512);
      const unsigned off = i16 < 8 ? H4_OFF + ((4 * xc + (wave >> 1)) * 4 + (i16 & 3)) * 256 + (64 * (wave & 1) + 32 * (i16 >> 2) + 8 * g) * 2 : OOB;
      unsigned r5w = lds0 + L_R5 + wave * 256 + lane * 4, r5r = lds0 + L_R5 + (lane >> 5) * 1024 + (lane & 31) * 8;
      unsigned h5off = lane < 32 ? H5_OFF + wave * 10240 + xc * 1280 + r * 128 + lane * 4 : OOB;
      KEEP(r5w); KEEP(r5r); KEEP(h5off);
      POLL1(v4u, a0, x = LD128(off), sent4(x), 2);
      float t[4];
#pragma unroll
      for (int tt = 0; tt < 4; ++tt) {
        const float s0 = sum4(mfma(a0, wo[0][tt], z4)), s1 = sum4(mfma(a0, wo[1][tt], z4));
        t[tt] = g == 0 ? s0 : s1;
      }
      const float u0 = tsum32(t[0], t[2]), u1 = tsum32(t[1], t[3]);
      *(LAS float*)(uintptr_t)r5w = tsum16(u0, u1);
      BARRIER();
      {
        const LAS char* a = (const LAS char*)(uintptr_t)r5r;
        const f2 s = (*(const LAS f2*)a + *(const LAS f2*)(a + 256)) + (*(const LAS f2*)(a + 512) + *(const LAS f2*)(a + 768));
        const unsigned v = pack_bf(tsum32(s.x, s.x), tsum32(s.y, s.y));
        ST32(v, h5off);
      }
    } else if (li < 27) {
      const bool three = wave < 4;
      const unsigned w1off = (unsigned)(lane >> 3) * 3456u + li * 128;
#pragma unroll
      for (int i = 0; i < 8; ++i)
        dma16(p.w1, w1off + (64 * wave + i * 8) * 3456u + (((lane & 7) ^ (((g & 1) | ((i & 1) << 1)) << 1)) * 16), lds0 + L_W1 + wave * 8192 + i * 1024);
      if (three) {
#pragma unroll
        for (int i = 0; i < 4; ++i)
          dma16(p.w1, w1off + (512 + 32 * wave + i * 8) * 3456u + (((lane & 7) ^ (((g & 1) | ((i & 1) << 1)) << 1)) * 16), lds0 + L_W2 + 65536 + wave * 4096 + i * 1024);
      }
      asm volatile("s_barrier" ::: "memory");
#pragma unroll
      for (int i = 0; i < 8; ++i) {
        const int n = i * 8 + wave, cb = n >> 4, rb = n & 15;
        dma16(p.w2, (unsigned)(64 * li + 4 * rb + g) * 1280u + cb * 256 + ((i16 ^ ((g | (((rb >> 1) & 1) << 2)) << 1)) * 16), lds0 + L_W2 + n * 1024);
      }
      v4u fix[3];
#pragma unroll
      for (int k = 0; k < 3; ++k) {
        const v4u xv4 = *(LAS v4u*)(L + L_X + (k < 2 ? 2 * wave + k : 16 + (wave & 3)) * 64 + g * 16);
        fix[k] = i16 == 8 ? xv4 : (v4u){0u, 0u, 0u, 0u};
      }
      const v4u ones = {ONE2, ONE2, ONE2, ONE2};
      VMWAIT(8);
      v4u b1[3][4];
      const int w1a = (8 * g + (i16 >> 2)) * 128 + (i16 & 3) * 8;
#pragma unroll
      for (int t = 0; t < 4; ++t) {
        b1[0][t] = frag(L + L_W1 + wave * 8192 + w1a + (t ^ f128) * 32, 512);
        b1[1][t] = frag(L + L_W1 + wave * 8192 + 4096 + w1a + (t ^ f128) * 32, 512);
        b1[2][t] = frag(L + L_W2 + 65536 + (wave & 3) * 4096 + w1a + (t ^ f128) * 32, 512);
      }
      {
        float t[4];
#pragma unroll
        for (int tt = 0; tt < 4; ++tt) {
          f4 d = mfma(ones, b1[0][tt], z4); d = mfma(ones, b1[1][tt], d);
          if (three) d = mfma(ones, b1[2][tt], d);
          t[tt] = d.x;
        }
        if (g == 0) *(LAS f4*)(L + L_C + wave * 256 + i16 * 16) = (f4){t[0], t[1], t[2], t[3]};
      }
      BARRIER();
#pragma unroll
      for (int i = 8; i < 10; ++i) {
        const int n = i * 8 + wave, cb = n >> 4, rb = n & 15;
        dma16(p.w2, (unsigned)(64 * li + 4 * rb + g) * 1280u + cb * 256 + ((i16 ^ ((g | (((rb >> 1) & 1) << 2)) << 1)) * 16), lds0 + L_W2 + n * 1024);
      }
      const int src = i16 >> 1, half = i16 & 1;
      f4 cb = *(LAS f4*)(L + L_C + i16 * 16);
#pragma unroll
      for (int w = 1; w < 8; ++w) cb += *(LAS f4*)(L + L_C + w * 256 + i16 * 16);
      cb *= -1.f / 640.f;
      KEEP(cb);
      {
        POLL1(v2u, cf, x = LD64(lane < 40 ? CF_OFF + lane * 8 : OOB), lane < 40 ? (x.x ^ tag) | (x.y ^ tag) : 0u, 8);
      }
      VMWAIT(0);
      asm volatile("s_barrier" ::: "memory");
      const int kh = wave >> 2, T0 = 10 * (wave & 3);
      v4u b2[10];
#pragma unroll
      for (int t = 0; t < 10; ++t) {
        const int T = T0 + t;
        b2[t] = frag(L + L_W2 + (T >> 3) * 16384 + (32 * kh + 8 * g + (i16 >> 2)) * 256 + (((T & 7) ^ ((i16 >> 2) | ((g & 1) << 2))) * 32) + (i16 & 3) * 8, 1024);
      }
      const unsigned hoff = H5_OFF + xc * 10240 + (i16 & 7) * 1280 + wave * 128 + (i16 >> 3) * 64 + g * 16;
      const unsigned hoff2 = (three && i16 < 8) ? H5_OFF + xc * 10240 + i16 * 1280 + 1024 + wave * 64 + g * 16 : OOB;
      unsigned sw = lds0 + L_S + wave * 256 + lane * 4, sr = lds0 + L_S + src * 256 + (32 * kh + 8 * g + 4 * half) * 4;
      unsigned ooff = H6_OFF + ((T0 >> 1) + (g >> 1)) * H6_STRIDE + (li * 2 + kh) * 128 + (lane & 31) * 4, ooff2 = lane < 32 ? ooff + 4 * H6_STRIDE : OOB;
      unsigned mk[4][2];
#pragma unroll
      for (int u = 0; u < 4; ++u) { mk[u][0] = i16 == 4 * u ? ~0u : 0u; mk[u][1] = i16 == 4 * u + 1 ? ~0u : 0u; KEEP(mk[u][0]); KEEP(mk[u][1]); }
      KEEP(sw); KEEP(sr); KEEP(ooff); KEEP(ooff2);
      POLL1(v4x2, hx, (x.a = LD128(hoff), x.b = LD128(hoff2)), (unsigned)(umax(max4(x.a), max4(x.b)) == SENT), 2);
      f4 d[4], d1s;
      {
        const v4u a0 = i16 < 8 ? hx.a : fix[0];
        d1s = mfma(a0, ones, z4);
#pragma unroll
        for (int tt = 0; tt < 4; ++tt) d[tt] = mfma(a0, b1[0][tt], z4);
        const v4u a1 = {low_from_high(fix[1].x, hx.a.x), low_from_high(fix[1].y, hx.a.y), low_from_high(fix[1].z, hx.a.z), low_from_high(fix[1].w, hx.a.w)};
        d1s = mfma(a1, ones, d1s);
#pragma unroll
        for (int tt = 0; tt < 4; ++tt) d[tt] = mfma(a1, b1[1][tt], d[tt]);
        if (three) {
          const v4u a2 = hx.b | fix[2];
          d1s = mfma(a2, ones, d1s);
#pragma unroll
          for (int tt = 0; tt < 4; ++tt) d[tt] = mfma(a2, b1[2][tt], d[tt]);
        }
        const float sh = sum4(d1s);
        const float u0 = tsum32(sum4(d[0]) + sh * cb.x, sum4(d[2]) + sh * cb.z), u1 = tsum32(sum4(d[1]) + sh * cb.y, sum4(d[3]) + sh * cb.w);
        *(LAS float*)(uintptr_t)sw = tsum16(u0, u1);
      }
      BARRIER();
      {
        const f4 s = *(LAS f4*)(uintptr_t)sr;
        float f[4];
#pragma unroll
        for (int e = 0; e < 4; ++e) {
          float u = s[e];
          u += dpp<QP_X2>(u); u += dpp<ROR8>(u); u += dpp<ROR4>(u);
          f[e] = fmx(u, 0.f);
        }
        const unsigned p0 = pack_bf(f[0], f[1]), p1 = pack_bf(f[2], f[3]);
        f4 q0 = z4, q1 = z4, q2 = z4;
#pragma unroll
        for (int u = 0; u < 4; ++u) {
          const v4u a = {p0 & mk[u][0], p1 & mk[u][0], p0 & mk[u][1], p1 & mk[u][1]};
          q0 = mfma(a, b2[u], q0); q1 = mfma(a, b2[4 + u], q1);
          if (u < 2) q2 = mfma(a, b2[8 + u], q2);
        }
        ST32(__float_as_uint(q2.x + q2.y), ooff2);
        ST32(__float_as_uint(q0.x + q0.y), ooff);
        ST32(__float_as_uint(q1.x + q1.y), ooff + 2 * H6_STRIDE);
      }
    } else {
      {
        const int nf = xc < 3 ? 4 : 3, ns = 6 - nf, si = r - 10 - nf;
        for (int n = si * 8 + wave; n < nf * 80; n += ns * 8) {
          const int c = n / nf, f = n - c * nf, cb = c >> 4, rb = c & 15;
          dma16(p.w2, (unsigned)(64 * (f * 8 + xc) + 4 * rb + g) * 1280u + cb * 256 + i16 * 16, lds0 + L_JUNK);
        }
      }
      if (li == 47) {
        const bool three = wave < 4;
        v4u fix[3];
#pragma unroll
        for (int k = 0; k < 3; ++k) {
          const v4u xv4 = *(LAS v4u*)(L + L_X + (k < 2 ? 2 * wave + k : 16 + (wave & 3)) * 64 + g * 16);
          fix[k] = (i16 == 8 && (k < 2 || three)) ? xv4 : (v4u){0u, 0u, 0u, 0u};
        }
        {
          POLL1(v2u, cf, x = LD64(lane < 40 ? CF_OFF + lane * 8 : OOB), lane < 40 ? (x.x ^ tag) | (x.y ^ tag) : 0u, 8);
        }
        const unsigned hoff = H5_OFF + xc * 10240 + (i16 & 7) * 1280 + wave * 128 + (i16 >> 3) * 64 + g * 16;
        const unsigned hoff2 = (three && i16 < 8) ? H5_OFF + xc * 10240 + i16 * 1280 + 1024 + wave * 64 + g * 16 : OOB;
        POLL1(v4x2, hx, (x.a = LD128(hoff), x.b = LD128(hoff2)), (unsigned)(umax(max4(x.a), max4(x.b)) == SENT), 2);
        const v4u ones = {ONE2, ONE2, ONE2, ONE2};
        const v4u a0 = i16 < 8 ? hx.a : fix[0];
        const v4u a1 = {low_from_high(fix[1].x, hx.a.x), low_from_high(fix[1].y, hx.a.y), low_from_high(fix[1].z, hx.a.z), low_from_high(fix[1].w, hx.a.w)};
        const v4u a2 = hx.b | fix[2];
        f4 gm = mfma(a0, a0, z4), sm = mfma(a0, ones, z4);
        gm = mfma(a1, a1, gm); sm = mfma(a1, ones, sm);
        gm = mfma(a2, a2, gm); sm = mfma(a2, ones, sm);
        const f2 part = {wave_sum(sum4(gm)), wave_sum(sum4(sm))};
        if (lane == 0) *(LAS f2*)(L + L_S + wave * 8) = part;
        BARRIER();
        const f4 t = (*(LAS f4*)(L + L_S) + *(LAS f4*)(L + L_S + 16)) + (*(LAS f4*)(L + L_S + 32) + *(LAS f4*)(L + L_S + 48));
        const float mean2 = (t.y + t.w) * (1.f / (16.f * 640.f));
        const unsigned rs = __float_as_uint(__builtin_amdgcn_rsqf(fmx((t.x + t.z) * (1.f / 640.f) - mean2 * mean2, 0.f) + 1e-5f));
        const int f = wave * 8 + (lane >> 3);
        if (f < 20) ST128(((v4u){rs, rs, rs, rs}), H6_OFF + f * H6_STRIDE + 54 * 128 + (lane & 7) * 16);
      } else if (wave < 2) {
        const int f = li - 27;
        float hres = 0.f;
        const int col = 4 * (lane & 3) + 2 * (lane >> 5) + (g & 1);
        unsigned uoff = H6_OFF + f * H6_STRIDE + (lane >> 2) * 128 + wave * 64 + (lane & 3) * 16, uoff3 = lane < 28 ? uoff + 6144 : OOB;
        {
          POLL1(v2u, cf, x = LD64(lane < 40 ? CF_OFF + lane * 8 : OOB), lane < 40 ? (x.x ^ tag) | (x.y ^ tag) : 0u, 8);
        }
        {
          const unsigned xw = *(LAS unsigned*)(L + L_X + f * 64 + wave * 32 + (lane & 7) * 4);
          const unsigned hoff = H5_OFF + xc * 10240 + (lane >> 3) * 1280 + f * 64 + wave * 32 + (lane & 7) * 4;
          POLL1(unsigned, hv, x = LD32(hoff), (unsigned)(x == SENT), 6);
          float h0 = bf_lo(hv), h1v = bf_hi(hv);
          h0 += dpp<ROR8>(h0); h1v += dpp<ROR8>(h1v);
          h0 = tsum16(h0, h0); h1v = tsum16(h1v, h1v);
          h0 = tsum32(h0, h0) + bf_lo(xw); h1v = tsum32(h1v, h1v) + bf_hi(xw);
          if (lane < 8) *(LAS f2*)(L + L_H + wave * 64 + lane * 8) = (f2){h0, h1v};
          asm volatile("s_waitcnt lgkmcnt(0)" ::: "memory");
          hres = *(LAS float*)(L + L_H + wave * 64 + col * 4);
        }
        unsigned keepm = lane >= 24 ? 0u : ~0u;
        GAS u16* op = (GAS u16*)p.out + 32 * f + 16 * wave + col;
        KEEP(hres); KEEP(uoff); KEEP(uoff3); KEEP(keepm); KEEP(op);
        if (FIN_DELAY) __builtin_amdgcn_s_sleep(FIN_DELAY);
        POLL1(v4x4, ux, (x.a = LD128(uoff), x.b = LD128(uoff + 2048), x.c = LD128(uoff + 4096), x.d = LD128(uoff3)),
              (unsigned)(umax(umax(max4(x.a), max4(x.b)), umax(max4(x.c), max4(x.d))) == SENT), 0);
        const float rs = __uint_as_float(__builtin_amdgcn_readlane(ux.d.x, 24));
        const f4 s = (__builtin_bit_cast(f4, ux.a) + __builtin_bit_cast(f4, ux.b)) + (__builtin_bit_cast(f4, ux.c) + __builtin_bit_cast(f4, ux.d & keepm));
        const float t0 = tsum32(s.x, s.z), t1 = tsum32(s.y, s.w);
        float t = tsum16(t0, t1);
        t += dpp<ROR8>(t); t += dpp<ROR4>(t);
        *op = (u16)pack_bf(t * rs + hres, 0.f);
        if (f == 0 && wave == 0 && lane == 0) ST32(epoch + 1u, 0);
      }
    }
  } else {
    const int v = wave & 3;
    const unsigned h3base = H3_OFF + ((sp_ * 4 + v) * 32 + hg * 16 + 4 * g) * 256 + i16 * 16;
    unsigned tag;
    const unsigned mloff = (wave ? LG_OFF : ML_OFF) + ((lane * 4 + j4) * 32 + h1) * 8;
    if (wave < 4) {
      v4u ka[2][16];
      VMWAIT(16);
      asm volatile("s_barrier" ::: "memory");
      tag = __builtin_amdgcn_readfirstlane(*(LAS unsigned*)(L + L_X));
#pragma unroll
      for (int s = 0; s < 16; ++s) ka[0][s] = *(LAS v4u*)(L + L_CKV + (32 * v + i16) * RS + 64 * s + 16 * g);
      VMWAIT(0);
#pragma unroll
      for (int s = 0; s < 16; ++s) ka[1][s] = *(LAS v4u*)(L + L_CKV + (32 * v + 16 + i16) * RS + 64 * s + 16 * g);
      unsigned qr = lds0 + L_Q + i16 * RS + 16 * g, mw_ = lds0 + L_M + i16 * 16 + v * 4, pw = lds0 + L_P + v * 1024 + lane * 16;
      unsigned lw_ = lds0 + L_LS + i16 * 64 + v * 16 + g * 4;
      unsigned aoff = v == 2 ? CA_OFF + ((h1 >> 4) * 64 + lane) * 128 : mloff & ~15u;
      KEEP(qr); KEEP(mw_); KEEP(pw); KEEP(lw_); KEEP(aoff);
      asm volatile("s_barrier" ::: "memory");
      BARRIER();
      f4 s0 = z4, s1 = z4;
      v4u qb[16];
#pragma unroll
      for (int s = 0; s < 16; ++s) qb[s] = *(LAS v4u*)(uintptr_t)(qr + 64 * s);
      SCHED();
#pragma unroll
      for (int s = 0; s < 16; ++s) {
        s0 = mfma(ka[0][s], qb[s], s0);
        s1 = mfma(ka[1][s], qb[s], s1);
      }
      float m = fmx(fmx(fmx(s0.x, s0.y), fmx(s0.z, s0.w)), fmx(fmx(s1.x, s1.y), fmx(s1.z, s1.w)));
      m = xmax32(xmax16(m));
      *(LAS float*)(uintptr_t)mw_ = m;
      BARRIER();
      const f4 mm = *(LAS f4*)(uintptr_t)(mw_ & ~15u);
      m = fmx(fmx(mm.x, mm.y), fmx(mm.z, mm.w));
      float e[8];
#pragma unroll
      for (int k = 0; k < 4; ++k) { e[k] = __builtin_amdgcn_exp2f(s0[k] - m); e[4 + k] = __builtin_amdgcn_exp2f(s1[k] - m); }
      const v4u pk = {pack_bf(e[0], e[1]), pack_bf(e[2], e[3]), pack_bf(e[4], e[5]), pack_bf(e[6], e[7])};
      *(LAS v4u*)(uintptr_t)pw = pk;
      *(LAS float*)(uintptr_t)lw_ = sum4((f4){e[0], e[1], e[2], e[3]} + (f4){e[4], e[5], e[6], e[7]});
      BARRIER();
      if (v == 2) {
        POLL1(unsigned, cf, x = LD32(aoff), x ^ tag, 2);
      } else if (v == 0) {
        __builtin_amdgcn_s_sleep(ML_DELAY);
        touch16(p.ws, aoff, lds0 + L_JUNK);
      }
    } else {
      tag = make_tag(__builtin_amdgcn_readfirstlane(LD32(0)));
      *(LAS unsigned*)(L + L_X) = tag;
      BARRIER();
      v2u vlo[8][4];
#pragma unroll
      for (int t = 0; t < 8; ++t)
#pragma unroll
        for (int ks = 0; ks < 4; ++ks)
          vlo[t][ks] = tr_read(L + L_CKV + (32 * ks + 4 * g + (i16 >> 2)) * RS + (8 * v + t) * 32 + (i16 & 3) * 8);
      const unsigned off = H2_OFF + xc * 32768 + (hg * 16 + 4 * v) * 1024 + lane * 16;
      if (v == 3) {
        __builtin_amdgcn_s_sleep(4);
        if (lane == 0) ST32(tag, CA_OFF + (hg * 64 + sp_) * 128);
      }
      if (v < 2) {
        POLL1(v2u, fv, x = LD64(F2_OFF + (v * 64 + lane) * 128), x.y ^ tag, 2);
        for (int sp = 0; sp < 1000; ++sp) {
          const unsigned now = (unsigned)__builtin_amdgcn_s_memrealtime();
          if (__builtin_amdgcn_ballot_w64(now - fv.x < F2_T) == 0ull) break;
          __builtin_amdgcn_s_sleep(1);
        }
      }
      asm volatile("s_barrier" ::: "memory");
      v4u qv[4];
#pragma unroll
      for (int n = 0; n < 4; ++n) qv[n] = LD128(off + n * 1024);
      PIN();
      v4u va[8][4];
#pragma unroll
      for (int t = 0; t < 8; ++t)
#pragma unroll
        for (int ks = 0; ks < 4; ++ks) {
          const v2u hi = tr_read(L + L_CKV + (32 * ks + 16 + 4 * g + (i16 >> 2)) * RS + (8 * v + t) * 32 + (i16 & 3) * 8);
          va[t][ks] = (v4u){vlo[t][ks].x, vlo[t][ks].y, hi.x, hi.y};
        }
      unsigned qw = lds0 + L_Q + 4 * v * RS + lane * 16;
      KEEP(qw);
      for (unsigned sp = 0;; ++sp) {
#pragma unroll
        for (int n = 0; n < 4; ++n) *(LAS v4u*)(uintptr_t)(qw + n * RS) = qv[n];
        if (__builtin_expect(__builtin_amdgcn_ballot_w64(umax(umax(max4(qv[0]), max4(qv[1])), umax(max4(qv[2]), max4(qv[3]))) == SENT) == 0ull, 1)) break;
        PIN();
        if (sp > SPIN_MAX) break;
        __builtin_amdgcn_s_sleep(3);
#pragma unroll
        for (int n = 0; n < 4; ++n) qv[n] = LD128(off + n * 1024);
      }
      BARRIER();
#pragma unroll
      for (int i = 0; i < 8; ++i)
        dma16(p.wuv, (unsigned)(128 * j4 + 32 * v + 4 * i + g) * 8192u + h1 * 256 + ((i16 ^ ((((2 * v + (i >> 2)) & 3) | ((g & 1) << 2)) << 1)) * 16),
              lds0 + L_WUV + (8 * v + i) * 1024);
      unsigned pr = lds0 + L_P + lane * 16, lr = lds0 + L_LS + i16 * 64 + g * 16;
      unsigned lgoff = lane < 16 ? LG_OFF + ((sp_ * 4 + v) * 32 + hg * 16 + i16) * 8 : OOB;
      unsigned h3st = h3base;
      KEEP(pr); KEEP(lr); KEEP(lgoff); KEEP(h3st);
      BARRIER();
      if (v == 0) {
        const f4 mm = *(LAS f4*)(L + L_M + i16 * 16);
        ST64(((v2u){__float_as_uint(fmx(fmx(mm.x, mm.y), fmx(mm.z, mm.w))), tag}), ML_OFF + ((sp_ * 4 + g) * 32 + hg * 16 + i16) * 8);
      }
      BARRIER();
      v4u pa[4];
#pragma unroll
      for (int ks = 0; ks < 4; ++ks) pa[ks] = *(LAS v4u*)(uintptr_t)(pr + ks * 1024);
      const f4 lp = *(LAS f4*)(uintptr_t)lr;
      SCHED();
      f4 o[8];
#pragma unroll
      for (int t = 0; t < 8; ++t) o[t] = mfma(pa[0], va[t][0], z4);
      {
        float l = sum4(lp);
        l = tsum16(l, l); l = tsum32(l, l);
        ST64(((v2u){__float_as_uint(l), tag}), lgoff);
      }
#pragma unroll
      for (int k = 0; k < 8; ++k) {
        __builtin_amdgcn_sched_group_barrier(0x8, 1, 0);
        __builtin_amdgcn_sched_group_barrier(0x2, 2, 0);
      }
      SCHED();
#pragma unroll
      for (int ks = 1; ks < 4; ++ks)
#pragma unroll
        for (int t = 0; t < 8; ++t) o[t] = mfma(pa[ks], va[t][ks], o[t]);
#pragma unroll
      for (int k = 0; k < 4; ++k) {
        const v4u ov = {pack_bf(o[0][k], o[1][k]), pack_bf(o[2][k], o[3][k]), pack_bf(o[4][k], o[5][k]), pack_bf(o[6][k], o[7][k])};
        ST128(ov, h3st + k * 256);
      }
      VMWAIT(4);
    }
    BARRIER();
    {
      const unsigned h3off = H3_OFF + (((8 * wave + g) * 4 + j4) * 32 + h1) * 256 + i16 * 16;
      if (S4_DELAY) __builtin_amdgcn_s_sleep(S4_DELAY);
      v4x2 hx;
      if (wave == 1) touch16(p.ws, mloff & ~15u, lds0 + L_LQ);
      hx.a = LD128(h3off); hx.b = LD128(h3off + 4 * 32 * 1024);
      PIN();
      v4u wb[2][4];
#pragma unroll
      for (int t = 0; t < 2; ++t)
#pragma unroll
        for (int ks = 0; ks < 4; ++ks)
          wb[t][ks] = frag(L + L_WUV + (16 * (i16 >> 2) + 4 * ks + g) * 256 + (((2 * v + t) ^ ((i16 >> 2) | ((g & 1) << 2))) * 32) + (i16 & 3) * 8, 64 * 256);
      unsigned ow = lds0 + L_O + wave * 288 + i16 * 16 + g * 4, ord = lds0 + L_O + i16 * 288 + 16 * g;
      unsigned h4off = !(lane & 17) ? H4_OFF + (h1 * 4 + j4) * 256 + wave * 64 + (lane >> 5) * 32 + i16 * 2 : OOB;
      unsigned wr = lds0 + L_ML + (8 * wave + g) * 4;
      KEEP(ow); KEEP(ord); KEEP(h4off); KEEP(wr);
      if (wave == 0) {
        VMWAIT(2);
        v2u lv = *(LAS v2u*)(L + L_JUNK + lane * 16 + (h1 & 1) * 8);
        if (__builtin_amdgcn_ballot_w64(lv.y != tag) != 0ull) {
          POLL1(v2u, mv, x = LD64(mloff), x.y ^ tag, 0);
          lv = mv;
        }
        const float ms = __uint_as_float(lv.x);
        *(LAS float*)(L + L_ML + lane * 4) = __builtin_amdgcn_exp2f(ms - wave_max(ms));
      }
      BARRIER();
      const float w0 = *(LAS float*)(uintptr_t)wr, w1 = *(LAS float*)(uintptr_t)(wr + 16);
      if (wave == 1) {
        VMWAIT(2);
        v2u lq = *(LAS v2u*)(L + L_LQ + lane * 16 + (h1 & 1) * 8);
        if (__builtin_amdgcn_ballot_w64(lq.y != tag) != 0ull) {
          POLL1(v2u, l2, x = LD64(mloff), x.y ^ tag, 0);
          lq = l2;
        }
        const float lt = wave_sum(*(LAS float*)(L + L_ML + lane * 4) * __uint_as_float(lq.x));
        *(LAS float*)(L + L_ML + 256) = __builtin_amdgcn_rcpf(lt);
      }
      for (unsigned sp = 0;; ++sp) {
        if (__builtin_expect(__builtin_amdgcn_ballot_w64(umax(max4(hx.a), max4(hx.b)) == SENT) == 0ull, 1)) break;
        PIN();
        if (sp > SPIN_MAX) break;
        hx.a = LD128(h3off); hx.b = LD128(h3off + 4 * 32 * 1024);
      }
      f2 o[4];
#pragma unroll
      for (int e = 0; e < 4; ++e) o[e] = (f2){bf_lo(hx.a[e]), bf_hi(hx.a[e])} * w0 + (f2){bf_lo(hx.b[e]), bf_hi(hx.b[e])} * w1;
      const float b0 = tsum32(o[0].x, o[2].x), b1 = tsum32(o[0].y, o[2].y), b2 = tsum32(o[1].x, o[3].x), b3 = tsum32(o[1].y, o[3].y);
      const float c0v = tsum16(b0, b2), c1v = tsum16(b1, b3);
      const unsigned ph = pack_bf(c0v, c1v);
      *(LAS unsigned*)(uintptr_t)ow = ph;
      *(LAS unsigned*)(uintptr_t)(ow + 8 * 288) = pack_bf(c0v - bf_lo(ph), c1v - bf_hi(ph));
      BARRIER();
      if (wave < 4) {
        v4u oa[4];
#pragma unroll
        for (int ks = 0; ks < 4; ++ks) oa[ks] = *(LAS v4u*)(uintptr_t)(ord + 64 * ks);
        const float linv = *(LAS float*)(L + L_ML + 256);
        SCHED();
        f4 d0 = mfma(oa[0], wb[0][0], z4), d1 = mfma(oa[0], wb[1][0], z4);
#pragma unroll
        for (int ks = 1; ks < 4; ++ks) { d0 = mfma(oa[ks], wb[0][ks], d0); d1 = mfma(oa[ks], wb[1][ks], d1); }
        float c = tsum32(sum4(d0), sum4(d1));
        c = tsum16(c, c) * linv;
        ST32(pack_bf(c, dpp<QP_X1>(c)), h4off);
      }
    }
  }
}

}

extern "C" void kernel_launch(void* const* d_in, const int* in_sizes, int n_in, void* d_out, int out_size, void* d_ws,
                              size_t ws_size, hipStream_t stream) {
  Params p;
  p.x = (const u16*)d_in[0];
  p.ckv = (const u16*)d_in[1];
  p.wq = (const u16*)d_in[2];
  p.wuk = (const u16*)d_in[3];
  p.wuv = (const u16*)d_in[4];
  p.wo = (const u16*)d_in[5];
  p.w1 = (const u16*)d_in[7];
  p.w2 = (const u16*)d_in[8];
  p.out = (u16*)d_out;
  p.ws = (char*)d_ws;
  mla_mlp_decode<<<256, 512, 0, stream>>>(p);
}
```
